```python
import torch

def _softmax(x, axis=-1):
    return torch.softmax(x, dim=axis)
def _gelu(x):
    return torch.nn.functional.gelu(x, approximate="tanh")
def _silu(x):
    return torch.nn.functional.silu(x)
def _sigmoid(x):
    return torch.sigmoid(x)
def _layernorm(x, axis=-1, eps=1e-5):
    m = x.mean(dim=axis, keepdim=True)
    v = x.var(dim=axis, keepdim=True, unbiased=False)
    return (x - m) / torch.sqrt(v + eps)
def _scatter_add(tgt, idx, src, axis=0):
    return tgt.clone().index_add_(axis, idx, src)
def _segment_sum(src, seg, n):
    out = torch.zeros((n,) + src.shape[1:], dtype=src.dtype)
    return out.index_add_(0, seg, src)
def _pad(x, pw):
    flat = tuple(v for pair in reversed(pw) for v in pair)
    return torch.nn.functional.pad(x, flat)

kernel_name = 'interleave_layers__ssm_conv_h3__dense_activation_relu__prompt'

def setup_inputs():

    _w = (lambda a: (lambda t: t.to(torch.bfloat16) if t.is_floating_point() else t)(torch.as_tensor(a)))
    def _A_setup():
        B, S, D = 1, 2048, 448
        ks = 4
        return {
            "q": _w((lambda *s: torch.randn(*s, dtype=torch.bfloat16) * 0.05)(B, S, D)),
            "k": _w((lambda *s: torch.randn(*s, dtype=torch.bfloat16) * 0.8)(B, S, D)),
            "v": _w((lambda *s: torch.randn(*s, dtype=torch.bfloat16) * 0.8)(B, S, D)),
            "shift_k": _w((lambda *s: torch.randn(*s, dtype=torch.bfloat16) * 0.8)(D, ks) * 0.5),
            "ssm_k": _w((lambda *s: torch.randn(*s, dtype=torch.bfloat16) * 0.8)(D, S) * (1.0 / S ** 0.5)),
            "ssm_D": _w((lambda *s: torch.randn(*s, dtype=torch.bfloat16) * 0.8)(D)),
        }
    _A_in = _A_setup()

    def _B_setup():
        B, S, D, Dff = 1, 2048, 448, 1792
        return {
            "x": _w((lambda *s: torch.randn(*s, dtype=torch.bfloat16) * 0.05)(B, S, D)),
            "w1": _w((lambda *s: torch.randn(*s, dtype=torch.bfloat16) * 0.1)(D, Dff)),
            "w2": _w((lambda *s: torch.randn(*s, dtype=torch.bfloat16) * 0.1)(Dff, D)),
        }
    _B_in = _B_setup()

    return {
        "A_q": _A_in["q"],
        "A_k": _A_in["k"],
        "A_v": _A_in["v"],
        "A_shift_k": _A_in["shift_k"],
        "A_ssm_k": _A_in["ssm_k"],
        "A_ssm_D": _A_in["ssm_D"],
        "B_x": _B_in["x"],
        "B_w1": _B_in["w1"],
        "B_w2": _B_in["w2"],
    }

def reference(A_q, A_k, A_v, A_shift_k, A_ssm_k, A_ssm_D, B_x, B_w1, B_w2):
    def _A_ref(q, k, v, shift_k, ssm_k, ssm_D):
        B, S, D = q.shape
        ks = shift_k.shape[-1]
        k_pad = _pad(k, ((0, 0), (ks - 1, 0), (0, 0)))
        k_shift = torch.zeros_like(k)
        for t in range(ks):
            k_shift = k_shift + k_pad[:, t:t + S, :] * shift_k[:, ks - 1 - t][None, None, :]
        kv = k_shift * v
        L = 2 * S
        kv_pad = _pad(kv, ((0, 0), (0, S), (0, 0)))
        kf_pad = _pad(ssm_k, ((0, 0), (0, S)))
        kv_f = (lambda x, axes=(-2,-1): torch.fft.rfft2(x.float(), dim=axes))(kv_pad[:, None, :, :], axes=(1, 2))
        kf_f = (lambda x, axes=(-2,-1): torch.fft.rfft2(x.float(), dim=axes))(kf_pad[:, None, :], axes=(1, 2))
        kf_f = (lambda x, *d: x.permute(*d))(kf_f, 1, 2, 0)
        y_f = kv_f * kf_f[None, :, :, :]
        y = (lambda x, s, axes=(-2,-1): torch.fft.irfft2(x, s=s, dim=axes).to(torch.bfloat16))(y_f, (1, L), axes=(1, 2))[:, 0, :S, :]
        y = y + kv * ssm_D[None, None, :]
        return q * y

    def _B_ref(x, w1, w2):
        h = torch.einsum("bsd,df->bsf", x, w1)
        zero = torch.zeros_like(h)
        h = torch.maximum(h, zero)
        return torch.einsum("bsf,fd->bsd", h, w2)

    x = A_q
    x = x + _A_ref(_layernorm(x, axis=-1), A_k, A_v, A_shift_k, A_ssm_k, A_ssm_D)
    x = x + _B_ref(_layernorm(x, axis=-1), B_w1, B_w2)
    x = x + _A_ref(_layernorm(x, axis=-1), A_k, A_v, A_shift_k, A_ssm_k, A_ssm_D)
    x = x + _B_ref(_layernorm(x, axis=-1), B_w1, B_w2)
    return x
```

```cpp
#include <hip/hip_runtime.h>
#include <hip/hip_bf16.h>
typedef unsigned short u16;
static constexpr int S = 2048, D = 448, F = 1792;
__device__ __forceinline__ float bf2f(u16 b) { return __uint_as_float((unsigned)b << 16); }
__device__ __forceinline__ u16 f2bf(float f) { unsigned u = __float_as_uint(f); u += 0x7fffu + ((u >> 16) & 1u); return (u16)(u >> 16); }
__device__ __forceinline__ float rb(float f) { return bf2f(f2bf(f)); }

__global__ void k_kv(const u16* k, const u16* v, const u16* sk, float* kvT) {
  int i = blockIdx.x * 256 + threadIdx.x; int s = i / D, d = i % D;
  float a = 0.f;
  for (int t = 0; t < 4; ++t) { int ss = s - 3 + t; float kk = ss >= 0 ? bf2f(k[ss * D + d]) : 0.f; a = rb(a + rb(kk * bf2f(sk[d * 4 + 3 - t]))); }
  kvT[d * S + s] = rb(a * bf2f(v[i]));
}
__global__ void k_conv(const float* kvT, const u16* h, const u16* Dv, float* y) {
  __shared__ float skv[S], sh[S];
  int d = blockIdx.x;
  for (int i = threadIdx.x; i < S; i += 256) { skv[i] = kvT[d * S + i]; sh[i] = bf2f(h[d * S + i]); }
  __syncthreads();
  float dd = bf2f(Dv[d]);
  for (int s = threadIdx.x; s < S; s += 256) {
    float a = 0.f;
    for (int t = 0; t <= s; ++t) a += skv[t] * sh[s - t];
    y[s * D + d] = rb(rb(a) + rb(skv[s] * dd));
  }
}
__device__ float bsum(float v, float* red) {
  for (int o = 32; o > 0; o >>= 1) v += __shfl_xor(v, o);
  return v;
}
__device__ void ln(const float* x, float* n) {
  float s = 0; for (int j = 0; j < 7; ++j) s += x[j];
  s = bsum(s, nullptr); float mean = s / D;
  float q = 0; for (int j = 0; j < 7; ++j) q += (x[j] - mean) * (x[j] - mean);
  q = bsum(q, nullptr); float var = rb(q / D); float m = rb(mean);
  float sd = rb(sqrtf(rb(var + 1e-5f)));
  for (int j = 0; j < 7; ++j) n[j] = rb(rb(x[j] - m) / sd);
}
__global__ void k_row(const u16* q, float* x, const float* f, const float* y, float* n, u16* out, int mode) {
  int s = blockIdx.x, l = threadIdx.x; float xv[7], nv[7];
  for (int j = 0; j < 7; ++j) { int i = s * D + l + 64 * j; xv[j] = mode == 0 ? bf2f(q[i]) : rb(x[i] + f[i]); }
  if (mode == 2) { for (int j = 0; j < 7; ++j) out[s * D + l + 64 * j] = f2bf(xv[j]); return; }
  ln(xv, nv);
  for (int j = 0; j < 7; ++j) { int i = s * D + l + 64 * j; xv[j] = rb(xv[j] + rb(nv[j] * y[i])); }
  ln(xv, nv);
  for (int j = 0; j < 7; ++j) { int i = s * D + l + 64 * j; x[i] = xv[j]; n[i] = nv[j]; }
}
__global__ void k_gemm(const float* A, const u16* B, float* C, int N, int K, int relu) {
  __shared__ float sa[16][17], sb[16][17];
  int r = blockIdx.y * 16 + threadIdx.y, c = blockIdx.x * 16 + threadIdx.x; float a = 0;
  for (int k0 = 0; k0 < K; k0 += 16) {
    sa[threadIdx.y][threadIdx.x] = A[r * K + k0 + threadIdx.x];
    sb[threadIdx.y][threadIdx.x] = bf2f(B[(k0 + threadIdx.y) * N + c]);
    __syncthreads();
    for (int k = 0; k < 16; ++k) a += sa[threadIdx.y][k] * sb[k][threadIdx.x];
    __syncthreads();
  }
  a = rb(a); if (relu) a = fmaxf(a, 0.f);
  C[r * N + c] = a;
}
extern "C" void kernel_launch(void* const* d_in, const int* in_sizes, int n_in, void* d_out, int out_size, void* d_ws, size_t ws_size, hipStream_t stream) {
  const u16 *q = (const u16*)d_in[0], *k = (const u16*)d_in[1], *v = (const u16*)d_in[2], *sk = (const u16*)d_in[3], *h = (const u16*)d_in[4], *Dv = (const u16*)d_in[5], *w1 = (const u16*)d_in[7], *w2 = (const u16*)d_in[8];
  float* ws = (float*)d_ws; float *kvT = ws, *y = kvT + S * D, *x = y + S * D, *n = x + S * D, *f = n + S * D, *H = f + S * D;
  k_kv<<<S * D / 256, 256, 0, stream>>>(k, v, sk, kvT);
  k_conv<<<D, 256, 0, stream>>>(kvT, h, Dv, y);
  k_row<<<S, 64, 0, stream>>>(q, x, f, y, n, (u16*)d_out, 0);
  k_gemm<<<dim3(F / 16, S / 16), dim3(16, 16), 0, stream>>>(n, w1, H, F, D, 1);
  k_gemm<<<dim3(D / 16, S / 16), dim3(16, 16), 0, stream>>>(H, w2, f, D, F, 0);
  k_row<<<S, 64, 0, stream>>>(q, x, f, y, n, (u16*)d_out, 1);
  k_gemm<<<dim3(F / 16, S / 16), dim3(16, 16), 0, stream>>>(n, w1, H, F, D, 1);
  k_gemm<<<dim3(D / 16, S / 16), dim3(16, 16), 0, stream>>>(H, w2, f, D, F, 0);
  k_row<<<S, 64, 0, stream>>>(q, x, f, y, n, (u16*)d_out, 2);
}
```

```cpp
#include <hip/hip_runtime.h>
#include <hip/hip_bf16.h>

namespace {
typedef unsigned short u16;
typedef unsigned u32;
typedef unsigned long long u64;
typedef short s16x4 __attribute__((ext_vector_type(4)));
typedef u32 v2u __attribute__((ext_vector_type(2)));
typedef u32 v4u __attribute__((ext_vector_type(4)));
typedef float f32x2 __attribute__((ext_vector_type(2)));
typedef float f32x4 __attribute__((ext_vector_type(4)));
typedef float f32x16 __attribute__((ext_vector_type(16)));
typedef __bf16 bf2 __attribute__((ext_vector_type(2)));
typedef __amdgpu_buffer_rsrc_t srd_t;
#define LAS __attribute__((address_space(3)))
#define DI __device__ __forceinline__
#define SB() __builtin_amdgcn_sched_barrier(0)
#define BAR() asm volatile("s_waitcnt lgkmcnt(0)\n\ts_barrier" ::: "memory")
#define BARE() asm volatile("s_barrier" ::: "memory")
#define VMW(n) asm volatile("s_waitcnt vmcnt(%0)" ::"i"(n) : "memory")
#define PIN(x) pin(x)
#define RARE(c) __builtin_expect(!!(c), 0)
#define SPIN(cap) _Pragma("nounroll") for (u32 sp_ = 0; sp_ < (cap); ++sp_)

constexpr int NB = 224;
constexpr u32 GSZ = 28672;
constexpr u32 GM = 64 * GSZ;
constexpr u32 HG = 114688;
constexpr u32 OFF_EPOCH = 0;
constexpr u32 OFF_DUMMY = 1024;
constexpr u32 OFF_FA = 4096;
constexpr u32 OFF_FB = OFF_FA + 32768;
constexpr u32 OFF_FW = OFF_FB + 32768;
constexpr u32 OFF_FY = OFF_FW + 32768;
constexpr u32 OFF_ST = OFF_FY + 65536;
constexpr u32 OFF_S2 = OFF_ST + 64 * 4 * 1024;
constexpr u32 OFF_QS = OFF_S2 + 64 * 4 * 1024;
constexpr u32 OFF_KV = OFF_QS + 16384;
constexpr u32 OFF_Q = OFF_KV + GM;
constexpr u32 OFF_X1 = OFF_Q + 2 * GM;
constexpr u32 OFF_X3 = OFF_X1 + GM;
constexpr u32 OFF_Y = OFF_X3 + GM;
constexpr u32 OFF_W2F = OFF_Y + GM;
constexpr u32 OFF_H = OFF_W2F + 1605632;
constexpr u32 WS_USED = OFF_H + 2 * 64 * HG;
constexpr u32 L_W1 = 0, L_HC1 = 114688, L_KI = L_HC1 + 16128, L_HC0 = L_KI + 512, L_VI = L_KI + 9216, L_QI = L_VI + 8192, L_WI = L_QI + 8192;
constexpr u32 L_KV = L_QI, L_RED = 114688;
constexpr u32 L_X = 0, L_R2 = 114688, L_SP = L_R2 + 34816, L_PS = L_SP + 1024, L_CS = L_PS + 2048, L_MN = L_CS + 512, L_DM = L_MN + 512, L_DM2 = L_DM + 1280;
constexpr u32 L_SY = 163840 - 256;
constexpr u32 LDS_BYTES = 163840;
constexpr u32 CODE_BYTES = 46080;
static_assert(L_WI + 7168 <= L_SY && L_DM2 + 8704 <= L_SY, "lds");
constexpr int W1A = 8, W1B = 12, W1D = 4, W1R = (56 - W1A - W1B) / 2, CK = 3, M0 = 20;
constexpr int TFP = 8, TFW = 40, TFB = 83, TQ = 84, TQW = 126;
constexpr bool cv_at(int t) { return t % CK == 1 && (t < TQ - 12 || t >= TQ + 16); }
constexpr int cv_sum(int lo, int hi) { int s = 0; for (int t = lo; t < hi; ++t) s += cv_at(t) ? 1 : 0; return s; }
constexpr int NC = cv_sum(0, 144), W2C = NC - (W1R - W1D), W2D = W2C, W2K = (110 - M0) / (56 - W2D);
constexpr u32 SPIN_CAP = 1u << 14;

struct Params { const u16 *q, *k, *v, *sk, *h, *Dv, *w1, *w2; u16* out; char* ws; };

DI float flo(u32 u) { return __uint_as_float(u << 16); }
DI float fhi(u32 u) { return __uint_as_float(u & 0xffff0000u); }
DI u32 pk(float a, float b) { f32x2 v = {a, b}; bf2 r = __builtin_convertvector(v, bf2); return __builtin_bit_cast(u32, r); }
DI float rb(float a) { return flo(pk(a, a)); }
DI float dot2(u32 a, u32 b, float c) { return __builtin_amdgcn_fdot2_f32_bf16(__builtin_bit_cast(bf2, a), __builtin_bit_cast(bf2, b), c, false); }
DI v2u tr(u32 a) { s16x4 r = __builtin_amdgcn_ds_read_tr16_b64_v4i16((LAS s16x4*)a); return __builtin_bit_cast(v2u, r); }
DI v4u tr2(u32 a, u32 b) { v2u l = tr(a), h = tr(b); return v4u{l.x, l.y, h.x, h.y}; }
DI v4u ld128(u32 a) { return *(LAS v4u*)a; }
DI f32x4 ldf4(u32 a) { return *(LAS f32x4*)a; }
DI void st128(u32 a, v4u v) { *(LAS v4u*)a = v; }
DI srd_t mk(const void* p, u32 n) { return __builtin_amdgcn_make_buffer_rsrc((void*)p, 0, n, 0x00020000); }
DI void gst(srd_t r, v4u v, u32 vo, u32 so) { asm volatile("buffer_store_dwordx4 %0, %1, %2, %3 offen sc1\n\ts_nop 1" ::"v"(v), "v"(vo), "s"(r), "s"(so) : "memory"); }
DI void gstp(srd_t r, v4u v, u32 vo, u32 so) { asm volatile("buffer_store_dwordx4 %0, %1, %2, %3 offen\n\ts_nop 1" ::"v"(v), "v"(vo), "s"(r), "s"(so) : "memory"); }
DI void gst32(srd_t r, u32 v, u32 vo) { asm volatile("buffer_store_dword %0, %1, %2, 0 offen sc1" ::"v"(v), "v"(vo), "s"(r) : "memory"); }
DI void gst64(srd_t r, v2u v, u32 vo) { asm volatile("buffer_store_dwordx2 %0, %1, %2, 0 offen sc1" ::"v"(v), "v"(vo), "s"(r) : "memory"); }
DI void lds_set(u32 a) { asm volatile("ds_write_b32 %0, %1" ::"v"(a), "v"(1u) : "memory"); }
DI void lds_wait(u32 a) { SPIN(1u << 22) { u32 f; asm volatile("ds_read_b32 %0, %1\n\ts_waitcnt lgkmcnt(0)" : "=&v"(f) : "v"(a) : "memory"); if (__builtin_amdgcn_readfirstlane(f)) break; } }
template <int IMM> DI void dma_p(u32 m, u32 vo, srd_t r, u32 so) { asm volatile("s_mov_b32 m0, %0\n\ts_nop 0\n\tbuffer_load_dwordx4 %1, %2, %3 offen offset:%4 lds" ::"s"(m), "v"(vo), "s"(r), "s"(so), "i"(IMM) : "memory"); }
template <int IMM> DI void dma_s(u32 m, u32 vo, srd_t r, u32 so) { asm volatile("s_mov_b32 m0, %0\n\ts_nop 0\n\tbuffer_load_dwordx4 %1, %2, %3 offen offset:%4 sc1 lds" ::"s"(m), "v"(vo), "s"(r), "s"(so), "i"(IMM) : "memory"); }
template <int Q> DI void dma_h(u32 m, u32 vo, srd_t r, u32 so) {
  if constexpr (Q) asm volatile("buffer_load_dwordx4 %0, %1, %2 offen offset:%3 sc1 lds" ::"v"(vo), "s"(r), "s"(so), "i"(Q * 1024) : "memory");
  else dma_s<0>(m, vo, r, so);
}
DI void DMA2(u32 m, u32 vo, srd_t r, u32 so) { asm volatile("s_mov_b32 m0, %0\n\ts_nop 0\n\tbuffer_load_dwordx4 %1, %2, %3 offen sc1 lds\n\tbuffer_load_dwordx4 %1, %2, %3 offen offset:1024 sc1 lds" ::"s"(m), "v"(vo), "s"(r), "s"(so) : "memory"); }
template <int IMM> DI void gldv_p(v4u& d, u32 vo, srd_t r, u32 so) { asm volatile("buffer_load_dwordx4 %0, %1, %2, %3 offen offset:%4" : "=v"(d) : "v"(vo), "s"(r), "s"(so), "i"(IMM) : "memory"); }
template <int IMM> DI void gldv_s(v4u& d, u32 vo, srd_t r, u32 so) { asm volatile("buffer_load_dwordx4 %0, %1, %2, %3 offen offset:%4 sc1" : "=v"(d) : "v"(vo), "s"(r), "s"(so), "i"(IMM) : "memory"); }
template <int IMM> DI void glda_s(v4u& d, u32 vo, srd_t r, u32 so) { asm volatile("buffer_load_dwordx4 %0, %1, %2, %3 offen offset:%4 sc1" : "=a"(d) : "v"(vo), "s"(r), "s"(so), "i"(IMM) : "memory"); }
DI void GLD32(u32& d, u32 vo, srd_t r, u32 so) { asm volatile("buffer_load_dword %0, %1, %2, %3 offen sc1" : "=v"(d) : "v"(vo), "s"(r), "s"(so) : "memory"); }
DI void mf_av(f32x16& c, const v4u& a, const v4u& b) { asm volatile("v_mfma_f32_32x32x16_bf16 %0, %1, %2, %0" : "+v"(c) : "a"(a), "v"(b)); }
DI void mf_avz(f32x16& c, const v4u& a, const v4u& b) { asm volatile("v_mfma_f32_32x32x16_bf16 %0, %1, %2, 0" : "=&v"(c) : "a"(a), "v"(b)); }
DI void mf_vv(f32x16& c, const v4u& a, const v4u& b) { asm volatile("v_mfma_f32_32x32x16_bf16 %0, %1, %2, %0" : "+v"(c) : "v"(a), "v"(b)); }
DI void MFMA16(f32x4& c, const v4u& a, const v4u& b) { asm volatile("v_mfma_f32_16x16x32_bf16 %0, %1, %2, %0" : "+v"(c) : "v"(a), "v"(b)); }
DI void nop12(f32x16& a, f32x16& b) { asm volatile("s_nop 11" : "+v"(a), "+v"(b)); }
DI void nop12(f32x16& a) { asm volatile("s_nop 11" : "+v"(a)); }
DI void toa(v4u& x) { asm volatile("" : "+a"(x)); }
DI void mf16z(f32x4& c, const v4u& a, const v4u& b) { asm volatile("v_mfma_f32_16x16x32_bf16 %0, %1, %2, 0" : "=&v"(c) : "v"(a), "v"(b)); }
template <class T> DI void pin(T& x) { asm volatile("" : "+v"(x)); }
template <int C> DI float dppadd(float x) { return x + __int_as_float(__builtin_amdgcn_update_dpp(0, __float_as_int(x), C, 0xf, 0xf, true)); }
DI float wave_sum(float x) {
  x = dppadd<0x111>(x); x = dppadd<0x112>(x); x = dppadd<0x114>(x); x = dppadd<0x118>(x);
  x = x + __int_as_float(__builtin_amdgcn_update_dpp(0, __float_as_int(x), 0x142, 0xa, 0xf, true));
  x = x + __int_as_float(__builtin_amdgcn_update_dpp(0, __float_as_int(x), 0x143, 0xc, 0xf, true));
  return __int_as_float(__builtin_amdgcn_readlane(__float_as_int(x), 63));
}
DI float half_sum(float x) { auto r = __builtin_amdgcn_permlane32_swap(__float_as_uint(x), __float_as_uint(x), false, false); return __uint_as_float(r[0]) + __uint_as_float(r[1]); }
DI void ln_consts(float s1, float s2, float& m, float& sd) {
  const float mean = s1 * (1.0f / 448.0f);
  const float var = fmaxf(s2 * (1.0f / 448.0f) - mean * mean, 0.0f);
  m = rb(mean);
  sd = rb(__builtin_amdgcn_sqrtf(rb(rb(var) + 1e-5f)));
}
DI int olane() { int l; asm volatile("v_mbcnt_lo_u32_b32 %0, -1, 0\n\tv_mbcnt_hi_u32_b32 %0, -1, %0" : "=v"(l)); return l; }
#define LANE_VARS const int lane = olane(), tid = w * 64 + lane, h2 = lane >> 5, blk = (lane >> 4) & 1, q4 = (lane & 15) >> 2, p4 = lane & 3; (void)tid; (void)h2; (void)blk; (void)q4; (void)p4
DI u32 sgnpat(u32 t) { return 0x8000u << ((t & 1u) * 16); }
DI float oct_sum(float x) {
  x = x + __int_as_float(__builtin_amdgcn_update_dpp(0, __float_as_int(x), 0x128, 0xf, 0xf, true));
  auto r = __builtin_amdgcn_permlane16_swap(__float_as_uint(x), __float_as_uint(x), false, false);
  return half_sum(__uint_as_float(r[0]) + __uint_as_float(r[1]));
}
DI void gld1(v4u& d, u32 vo, srd_t r, u32 so) { asm volatile("s_nop 4\n\tbuffer_load_dwordx4 %0, %1, %2, %3 offen sc1" : "=v"(d) : "v"(vo), "s"(r), "s"(so) : "memory"); }
DI u32 nosent(u32 x) { return x == ~0u ? 0x7fc07fc0u : x; }

struct ConvSched { int sp[144], d[144], res[144], issue[32], first[145]; };
constexpr ConvSched make_sched() {
  ConvSched c{}; int t = 0;
  for (int r = 0; r < 4; ++r)
    for (int m = 0; m < 8; ++m) {
      c.issue[r * 8 + m] = t - 9 < 0 ? 0 : t - 9;
      for (int o = 0; o <= m; ++o) {
        if (m + o > 7) break;
        c.sp[t] = m; c.d[t] = o; c.res[t] = r; ++t;
        if (o != m) { c.sp[t] = o; c.d[t] = m; c.res[t] = r; ++t; }
      }
    }
  for (int u = 0, n = 0; u <= 144; ++u) { while (n < 32 && c.issue[n] < u) ++n; c.first[u] = n; }
  return c;
}
constexpr ConvSched CS = make_sched();
template <int V> struct IC { static constexpr int value = V; };
template <int I, int N, class Fn> DI void sfor(Fn&& f) { if constexpr (I < N) { f(IC<I>{}); sfor<I + 1, N>(f); } }

struct Epi {
  f32x4 pa, c[2]; float s1, s2, mean, mb, t0, t1, u0, u1; u32 so, mx, ng; v4u o;
};
DI float add_v(float a, float b) { float r; asm volatile("v_add_f32 %0, %1, %2" : "=v"(r) : "v"(a), "v"(b)); return r; }
DI float mul_v(float a, float b) { float r; asm volatile("v_mul_f32 %0, %1, %2" : "=v"(r) : "v"(a), "v"(b)); return r; }
DI float fma_v(float a, float b, float c) { float r; asm volatile("v_fma_f32 %0, %1, %2, %3" : "=v"(r) : "v"(a), "v"(b), "v"(c)); return r; }
DI float fnma_v(float a, float b, float c) { float r; asm volatile("v_fma_f32 %0, -%1, %2, %3" : "=v"(r) : "v"(a), "v"(b), "v"(c)); return r; }
DI u32 pk_v(float a, float b) { u32 r; asm volatile("v_cvt_pk_bf16_f32 %0, %1, %2" : "=v"(r) : "v"(a), "v"(b)); return r; }
DI u32 sgn_v(u32 x, u32 sel) { u32 r; asm volatile("v_perm_b32 %0, %1, %1, %2" : "=v"(r) : "v"(x), "s"(sel)); return r; }
DI u32 relu_tag_v(u32 x, u32 neg, u32 m) { u32 r; asm volatile("v_bitop3_b32 %0, %1, %2, %3 bitop3:0xba" : "=v"(r) : "v"(x), "v"(neg), "s"(m)); return r; }
DI u32 csel(u64 c, u32 a, u32 b) { u32 r; asm volatile("s_cmp_lg_u64 %1, 0\n\ts_cselect_b32 %0, %2, %3" : "=s"(r) : "s"(c), "s"(a), "s"(b) : "scc"); return r; }
DI u32 shl16_v(u32 a) { u32 r; asm volatile("v_lshlrev_b32 %0, 16, %1" : "=v"(r) : "v"(a)); return r; }
DI u32 and_v(u32 a, u32 m) { u32 r; asm volatile("v_and_b32 %0, %2, %1" : "=v"(r) : "v"(a), "s"(m)); return r; }
DI u32 off_v(u32 x, u32 y, u32 m) { u32 r; asm volatile("v_bitop3_b32 %0, %1, %2, %3 bitop3:0x7e" : "=v"(r) : "v"(x), "v"(y), "s"(m)); return r; }
DI u32 or3_v(u32 a, u32 x, u32 y) { u32 r; asm volatile("v_or3_b32 %0, %1, %2, %3" : "=v"(r) : "v"(a), "v"(x), "v"(y)); return r; }
constexpr bool w2_at(int m) { return m >= M0 && (m - M0) % W2K == 0 && (m - M0) / W2K < 56 - W2D; }
constexpr int M_FY = 30, M_Y = 58, M_SQ = 91, M_HB = 8, M_XB = 22;
constexpr int g1_ops(int L, int m) {
  const int sl = m / 28, i = m % 28;
  return (sl == 3 && i % 2 == 0 ? 1 : 0) +
         (L == 0 ? (w2_at(m) ? 1 : 0) + (m == M_FY ? 1 : 0) + (m == M_Y ? 2 : 0) + (m < 14 ? 1 : 0) : (m == M_HB ? 1 : 0) + (m >= M_XB && m < M_XB + 14 ? 1 : 0));
}
constexpr int g1_sum(int L, int lo, int hi) { int s = 0; for (int m = lo; m < hi; ++m) s += g1_ops(L, m); return s; }
constexpr int U_S2 = 11, EA0 = 6, U_EA = 1, U_PA = 12;
constexpr int g2_ops(int L, int st, int u) {
  if (u < 0) return 0;
  return (st < 3 ? 1 : 0) + (st == 0 && u == U_S2 ? 1 : 0) + (L == 0 && st == 3 && u == U_PA ? 1 : 0);
}
constexpr int g2_sum(int L, int st, int lo, int hi) { int s = 0; for (int u = lo < 0 ? 0 : lo; u < hi; ++u) s += g2_ops(L, st, u); return s; }
constexpr int h_wait(int L, int st, int i) {
  const int before = st == 0 ? g1_sum(L, 84 + 2 * i + 1, 112) : g2_sum(L, st - 1, i + 1, 14);
  return before + g2_sum(L, st, 0, i - 2);
}

__global__ void __launch_bounds__(256, 1) h3ffn_kernel(Params P) {
  u64 pc; u32 cpf;
  asm volatile("s_getpc_b64 %0" : "=s"(pc));
  asm volatile("v_lshlrev_b32 %0, 7, %1\n\tglobal_load_dword %0, %0, %2" : "=&v"(cpf) : "v"(threadIdx.x), "s"(pc) : "memory");
  const int tid = threadIdx.x, lane = tid & 63;
  const int w = __builtin_amdgcn_readfirstlane(tid >> 6);
  const int b = blockIdx.x, xcd = b & 7, j = b >> 3;
  const int ct = j % 7, tt = xcd * 4 + j / 7, T1 = xcd * 2 + j / 14, hf = (j / 7) & 1, ft = j % 14;
  const int s0 = tt * 64;
  if (RARE(b >= NB)) {
    const srd_t rC = mk((const void*)(pc & ~127ull), CODE_BYTES), rX = mk(P.w1, 1605632);
    const u32 vo = ((j - 28) * 256 + tid) * 128;
    const srd_t rS = mk(P.ws, WS_USED);
    u32 d, e;
    asm volatile("s_nop 4\n\tbuffer_load_dword %0, %1, %2, 0 offen" : "=v"(d) : "v"(vo + 32768), "s"(rC) : "memory");
    asm volatile("buffer_load_dword %0, off, %1, 0 sc1" : "=v"(e) : "s"(rS) : "memory");
#pragma unroll
    for (int n = 0; n < 13; ++n) asm volatile("buffer_load_dword %0, %1, %2, %3 offen" : "=v"(d) : "v"(vo), "s"(rX), "s"((u32)(((n + xcd * 13 / 8) % 13) * 131072)) : "memory");
    VMW(0); PIN(e); PIN(cpf);
    SPIN(24u) {
      u32 f[4];
#pragma unroll
      for (int i = 0; i < 4; ++i) GLD32(f[i], (lane + 64 * i < NB ? lane + 64 * i : 0) * 128 + OFF_FW, rS, 0u);
      VMW(0); PIN(f[0]); PIN(f[1]); PIN(f[2]); PIN(f[3]);
      if (__all(f[0] == e + 1u && f[1] == e + 1u && f[2] == e + 1u && f[3] == e + 1u)) {
#pragma unroll
        for (int n = 0; n < 13; ++n) if (n < 12 || vo < 32768) asm volatile("buffer_load_dword %0, %1, %2, %3 offen sc1" : "=v"(d) : "v"(vo), "s"(rS), "s"((u32)(OFF_W2F + n * 131072)) : "memory");
        VMW(0);
        break;
      }
      __builtin_amdgcn_s_sleep(32);
    }
    return;
  }
  const srd_t rW = mk(P.ws, WS_USED);
  v4u W1[28], W2[56];
  const int h2 = lane >> 5, blk = (lane >> 4) & 1, q4 = (lane & 15) >> 2, p4 = lane & 3;
  const u32 vL = lane * 16;
  u32 Ev, Dw, sW1, mW1, fw, vfw_;
  v4u qg[4], qs[4]; v2u qd[4];
  const srd_t rW1 = mk(P.w1, 1605632);
  const u32 v1 = (lane >> 3) * 3584 + (lane & 7) * 16;
  {
    const srd_t rK = mk(P.k, GM), rV = mk(P.v, GM), rSK = mk(P.sk, 3584), rD = mk(P.Dv, 896), rH = mk(P.h, GM), rQ = mk(P.q, GM), rW2 = mk(P.w2, 1605632);
    const u32 vt = (lane >> 3) * 896 + (lane & 7) * 16;
    const int G = lane >> 4;
    const u32 ab = (8 * (p4 & 1) + q4) * 128 + (8 * G + 4 * (p4 >> 1)) * 2;
    const u32 bo = (8 * G + 4 * (q4 >> 1) + p4) * 32 + (q4 & 1) * 16;
#define W1P(n) dma_p<0>(L_W1 + (n) * 1024, v1, rW1, ft * 256 + ((n) >= 56 ? 128 - 56 * 8 * 3584 : 0) + (n) * 8 * 3584)
    const int pw = (w & 1) * 56;
    v2u taps[2]; v4u hc[4], rows[5];
    const int rbase = s0 + (ct * 9 < 54 ? ct * 9 : 54) + (w >> 1) * 5;
    {
      const int i0 = (tt == 0 && w == 0) ? 8 : w;
      asm volatile("s_nop 4");
      dma_p<0>(L_KI + i0 * 1024, vt, rK, (u32)((s0 - 8 + 8 * i0) * 896 + ct * 128));
      dma_p<0>(L_KI + (w + 4) * 1024, vt, rK, (u32)((s0 + 24 + 8 * w) * 896 + ct * 128));
      dma_p<0>(L_VI + w * 2048, vt, rV, (u32)((s0 + 16 * w) * 896 + ct * 128));
      dma_p<0>(L_VI + w * 2048 + 1024, vt, rV, (u32)((s0 + 16 * w + 8) * 896 + ct * 128));
      const u32 vch = (ct * 64 + G * 8 + (q4 >> 1) * 4 + p4) * 8;
      asm volatile("buffer_load_dwordx2 %0, %1, %2, 0 offen" : "=v"(taps[0]) : "v"(vch), "s"(rSK) : "memory");
      asm volatile("buffer_load_dwordx2 %0, %1, %2, 0 offen offset:256" : "=v"(taps[1]) : "v"(vch), "s"(rSK) : "memory");
      if (w == 0) dma_p<0>(L_KI + 8192, vt, rK, (u32)((s0 + 56) * 896 + ct * 128));
      asm volatile("buffer_load_dword %0, off, %1, 0 sc1" : "=v"(Ev) : "s"(rW) : "memory");
      asm volatile("buffer_load_dword %0, off, %1, %2" : "=v"(Dw) : "s"(rD), "s"((u32)(b * 4)) : "memory");
      if (tid == 0) { st128(L_SY, v4u{0, 0, 0, 0}); st128(L_SY + 16, v4u{0, 0, 0, 0}); }
      if (tt == 0 && w == 0) st128(L_KI + vL, v4u{0, 0, 0, 0});
      const u32 vr = (lane < 56 ? lane : 55) * 16, sh = b * 8192 + (w & 1) * 4096;
      if (w == 0) VMW(2);
      else if (w == 1) {
        const u32 vw = (lane >> 2) * 896 + (lane & 3) * 16;
#pragma unroll
        for (int n = 0; n < 7; ++n) dma_p<0>(L_WI + n * 1024, vw, rW2, (u32)(((b / 14) * 112 + 16 * n) * 896 + (b % 14) * 64));
#pragma unroll
        for (int i = 0; i < 5; ++i) gldv_p<0>(rows[i], vr, rQ, (u32)((rbase + i) * 896));
        VMW(14);
      } else if (w == 2) {
#pragma unroll
        for (int i = 0; i < 8; ++i) dma_p<0>(L_QI + i * 1024, vt, rQ, (u32)((s0 + 8 * i) * 896 + ct * 128));
        gldv_p<0>(hc[0], vL, rH, sh); gldv_p<1024>(hc[1], vL, rH, sh); gldv_p<2048>(hc[2], vL, rH, sh); gldv_p<3072>(hc[3], vL, rH, sh);
#pragma unroll
        for (int n = 0; n < W1A; ++n) W1P(pw + n);
        VMW(14 + W1A);
      } else {
        gldv_p<0>(hc[0], vL, rH, sh); gldv_p<1024>(hc[1], vL, rH, sh); gldv_p<2048>(hc[2], vL, rH, sh); gldv_p<3072>(hc[3], vL, rH, sh);
#pragma unroll
        for (int i = 0; i < 5; ++i) gldv_p<0>(rows[i], vr, rQ, (u32)((rbase + i) * 896));
#pragma unroll
        for (int n = 0; n < W1A; ++n) W1P(pw + n);
        VMW(11 + W1A);
      }
      PIN(taps[0]); PIN(taps[1]);
    }
    BAR();
    {
      v4u kvo[2];
#pragma unroll
      for (int it = 0; it < 2; ++it) {
        const float t0 = flo(taps[it].x), t1 = fhi(taps[it].x), t2 = flo(taps[it].y), t3 = fhi(taps[it].y);
        const u32 ak = L_KI + w * 2048 + ab + 64 * it, av = L_VI + w * 2048 + ab + 64 * it;
        const v2u ka = tr(ak + 512), kb = tr(ak + 1024), kc = tr(ak + 1536);
        const v4u vv = tr2(av, av + 512);
        const float kf[11] = {fhi(ka.x), flo(ka.y), fhi(ka.y), flo(kb.x), fhi(kb.x), flo(kb.y), fhi(kb.y), flo(kc.x), fhi(kc.x), flo(kc.y), fhi(kc.y)};
        float o[8];
#pragma unroll
        for (int i = 0; i < 8; ++i) {
          const float ks = (kf[i] * t3 + kf[i + 1] * t2) + (kf[i + 2] * t1 + kf[i + 3] * t0);
          o[i] = ks * ((i & 1) ? fhi(vv[i >> 1]) : flo(vv[i >> 1]));
        }
        kvo[it] = v4u{pk(o[0], o[1]), pk(o[2], o[3]), pk(o[4], o[5]), pk(o[6], o[7])};
      }
      PIN(kvo[0]); PIN(kvo[1]);
      asm volatile("" ::: "memory");
      st128(L_VI + w * 2048 + bo, kvo[0]); st128(L_VI + w * 2048 + bo + 1024, kvo[1]);
    }
    BAR();
    auto qstats = [&](u32 tg) {
      float z[10];
#pragma unroll
      for (int i = 0; i < 5; ++i) {
        PIN(rows[i]);
        float a1 = 0.f, a2 = 0.f;
#pragma unroll
        for (int e = 0; e < 4; ++e) { const u32 u = lane < 56 ? rows[i][e] : 0u; a1 = dot2(u, 0x3f803f80u, a1); a2 = dot2(u, u, a2); }
        z[i] = a1; z[5 + i] = a2;
      }
#pragma unroll
      for (int i = 0; i < 10; ++i) z[i] = wave_sum(z[i]);
      float z1 = z[0], z2 = z[5];
#pragma unroll
      for (int i = 1; i < 5; ++i) { z1 = lane == i ? z[i] : z1; z2 = lane == i ? z[5 + i] : z2; }
      float m, sd; ln_consts(z1, z2, m, sd);
      if (lane < 5) { const u32 s = rbase + lane; gst64(rW, v2u{pk(m, sd), tg}, ((s >> 1) & 3) * 4096 + (s >> 3) * 16 + (s & 1) * 8 + OFF_QS); }
    };
    auto hcopies = [&]() {
      const u32 hb = (w & 1) ? L_HC1 : L_HC0;
#pragma unroll
      for (int i = 0; i < 4; ++i) {
        PIN(hc[i]);
        u32 e[6];
#pragma unroll
        for (int x = 0; x < 2; ++x) e[x] = __builtin_amdgcn_update_dpp(i ? __builtin_amdgcn_readlane(hc[i ? i - 1 : 0][2 + x], 63) : 0, hc[i][2 + x], 0x138, 0xf, 0xf, false);
#pragma unroll
        for (int x = 0; x < 4; ++x) e[2 + x] = hc[i][x];
        u32 zd[5], zo[5];
#pragma unroll
        for (int x = 0; x < 5; ++x) { zd[x] = __builtin_amdgcn_alignbit(e[5 - x], e[5 - x], 16); zo[x] = (e[5 - x] & 0xffffu) | (e[4 - x] & 0xffff0000u); }
        const u32 a = hb + 4080 - 16 * (lane + 64 * i);
        st128(a, v4u{zd[0], zd[1], zd[2], zd[3]}); st128(a + 4160, v4u{zo[0], zo[1], zo[2], zo[3]});
        st128(a + 8320, v4u{zd[1], zd[2], zd[3], zd[4]}); st128(a + 12480, v4u{zo[1], zo[2], zo[3], zo[4]});
      }
      if (lane < 8) st128(hb + (lane >> 1) * 4160 + 4096 + (lane & 1) * 16, v4u{0, 0, 0, 0});
    };
    const u32 agk = ((lane >> 1) & 3) * 2048 + (lane >> 3) * 32 + (lane & 1) * 16 + w * 512;
    const u32 agr = ((lane >> 1) & 3) * 2048 + (lane >> 4) * 32 + (lane & 1) * 16 + ((lane >> 3) & 1) * 8 + w * 512;
    {
      const u32 sg = OFF_KV + (ct * 64 + w * 16) * 4096 + tt * 128, vs = (lane >> 3) * 4096 + (lane & 7) * 16;
      gst(rW, ld128(L_VI + agk), vs, sg); gst(rW, ld128(L_VI + agk + 256), vs, sg + 32768);
      if (lane == 0) asm volatile("ds_add_u32 %0, %1" ::"v"(L_SY + 4), "v"(1u) : "memory");
    }
#define KVDONE() do { if (lane == 0) asm volatile("ds_add_u32 %0, %1" ::"v"(L_SY + 16), "v"(1u) : "memory"); } while (0)
#define LDSEQ(a, n) SPIN(1u << 22) { u32 f_; asm volatile("ds_read_b32 %0, %1\n\ts_waitcnt lgkmcnt(0)" : "=&v"(f_) : "v"(a) : "memory"); if (__builtin_amdgcn_readfirstlane(f_) == (n)) break; }
#define QREAD() do { lds_wait(L_SY + 28); _Pragma("unroll") for (int i = 0; i < 4; ++i) qd[i] = *(LAS v2u*)(L_QI + agr + i * 128); \
      _Pragma("unroll") for (int i = 0; i < 4; ++i) PIN(qd[i]); if (lane == 0) asm volatile("ds_add_u32 %0, %1" ::"v"(L_SY + 8), "v"(1u) : "memory"); } while (0)
    const u32 vfw = (((lane & 31) >> 1) * 14 + 2 * ct + (lane & 1)) * 128 + OFF_FW;
    if (__builtin_expect(w == 0, 1)) {
      VMW(0); PIN(Ev);
      const u32 tg = Ev + 1u;
      LDSEQ(L_SY + 16, 3u);
      if (lane == 0) gst32(rW, tg, b * 128 + OFF_FA);
      const u32 va = (((lane & 31) >> 2) + 8 * (7 * (lane & 3) + (b >> 5))) * 128 + OFF_FA;
      QREAD();
      LDSEQ(L_SY + 8, 4u); lds_wait(L_SY + 12);
      st128(L_KV + h2 * 4608 + (lane & 31) * 16, v4u{0, 0, 0, 0});
      SPIN(SPIN_CAP) { u32 f; GLD32(f, va, rW, 0u); VMW(0); PIN(f); if (__all(f == tg)) break; }
#pragma unroll
      for (int c = 0; c < 2; ++c) { DMA2(L_KV + c * 4608 + 512, vL, rW, OFF_KV + b * 8192 + c * 4096); DMA2(L_KV + c * 4608 + 2560, vL, rW, OFF_KV + b * 8192 + c * 4096 + 2048); }
      if (lane == 0) lds_set(L_SY + 24);
    } else if (w == 1) {
      VMW(5); PIN(Ev);
      const u32 tg = Ev + 1u;
      v4u wf[7];
      const u32 aw = L_WI + (4 * h2 + q4) * 64 + blk * 32 + p4 * 8;
#pragma unroll
      for (int n = 0; n < 7; ++n) wf[n] = tr2(aw + n * 1024, aw + n * 1024 + 512);
      const u32 sm = sgnpat(tg);
      const int c32g = b % 14;
#pragma unroll
      for (int n = 0; n < 7; ++n) {
        const int ks16 = 7 * (b / 14) + n, ft32 = ks16 >> 1;
        gst(rW, wf[n] ^ sm, vL, OFF_W2F + (((((c32g >> 1) * 4 + (ft32 & 3)) * 14 + (ft32 >> 2)) * 2 + (ks16 & 1)) * 2 + (c32g & 1)) * 1024);
      }
      if (lane == 0) lds_set(L_SY + 12);
      VMW(7);
      KVDONE();
      QREAD();
      VMW(0);
      if (lane == 0) gst32(rW, tg, b * 128 + OFF_FW);
      qstats(tg);
      lds_wait(L_SY + 24);
    } else if (w == 2) {
      VMW(4 + W1A);
      v4u qo[8];
#pragma unroll
      for (int i = 0; i < 8; ++i) { const u32 a = L_QI + (i >> 1) * 2048 + ab + 64 * (i & 1); qo[i] = tr2(a, a + 512); }
#pragma unroll
      for (int i = 0; i < 8; ++i) PIN(qo[i]);
      asm volatile("" ::: "memory");
#pragma unroll
      for (int i = 0; i < 8; ++i) st128(L_QI + i * 1024 + bo, qo[i]);
      asm volatile("s_waitcnt lgkmcnt(0)" ::: "memory");
      if (lane == 0) lds_set(L_SY + 28);
      QREAD();
      VMW(0);
      KVDONE();
      LDSEQ(L_SY + 4, 4u);
      hcopies();
      lds_wait(L_SY + 24);
    } else {
      VMW(0); PIN(Ev);
      KVDONE();
#pragma unroll
      for (int n = W1A; n < W1A + 2 * W1B; ++n) W1P(n < W1A + W1B ? 56 + n : n - W1B);
      QREAD();
      qstats(Ev + 1u);
      LDSEQ(L_SY + 4, 4u);
      hcopies();
      lds_wait(L_SY + 24);
    }
    if (w == 0) VMW(0);
    {
      PIN(Ev);
      const u32 tg = Ev + 1u;
#pragma unroll
      for (int i = 0; i < 4; ++i) gst(rW, v4u{qd[i].x, tg, qd[i].y, tg}, (lane >> 3) * 4096 + (lane & 7) * 16, OFF_Q + (ct * 64 + w * 16 + i * 4) * 8192 + tt * 128);
#pragma unroll
      for (int i = 0; i < 2; ++i) gst(rW, v4u{~0u, ~0u, ~0u, ~0u}, (lane >> 3) * GSZ + (lane & 7) * 16, OFF_X1 + (2 * w + i) * 8 * GSZ + b * 128);
    }
    static_assert(W1R * 2 + W1A + W1B == 56 && W1D <= W1R, "W1 split");
    sW1 = ft * 256 + (w & 1) * 128 + (W1A + W1B + (w >> 1) * W1R) * 8 * 3584; mW1 = L_W1 + (pw + W1A + W1B + (w >> 1) * W1R) * 1024;
#pragma unroll
    for (int n = 0; n < W1D; ++n) dma_p<0>(mW1 + n * 1024, v1, rW1, sW1 + n * 8 * 3584);
    vfw_ = vfw;
  }
  PIN(Ev); PIN(Dw); PIN(cpf);
  const u32 tag = __builtin_amdgcn_readfirstlane(Ev) + 1u;
  const u32 sm = sgnpat(tag), smB = sm ^ 0x80008000u;
#define W2LD(n) do { constexpr int pn_ = (n) >> 2; const u32 so_ = OFF_W2F + (ct * 4 + w) * 57344 + (pn_ < 7 ? 2 * pn_ + hf : 2 * (pn_ - 7) + 1 - hf) * 4096; \
    if constexpr ((n) < 36) glda_s<((n) & 3) * 1024>(W2[n], vL, rW, so_); else gldv_s<((n) & 3) * 1024>(W2[n], vL, rW, so_); } while (0)
  BAR();
  {
    v4u qv[2];
    const u32 vg = (tid >> 2) * GSZ + (tid & 3) * 16;
    const int c = w >> 1, rr = w & 1, cc = lane & 15, G = lane >> 4, be = 15 - cc;
    const u32 aT = (c ? L_HC1 : L_HC0) + (be & 3) * 4160 + 2 * (be & 12) + 16 * G + 256 * (1 - rr);
    u32 aT2 = aT + 8; PIN(aT2);
    const u32 aF = L_KV + c * 4608 + 32 * cc + 16 * G + 256 * (1 - rr) + 32;
    f32x4 acc[8];
#pragma unroll
    for (int i = 0; i < 8; ++i) acc[i] = f32x4{0.f, 0.f, 0.f, 0.f};
    asm volatile("s_nop 1" : "+v"(acc[0]), "+v"(acc[1]), "+v"(acc[2]), "+v"(acc[3]), "+v"(acc[4]), "+v"(acc[5]), "+v"(acc[6]), "+v"(acc[7]));
    v4u A[32], Fm[32];
    float nq[2][8];
    sfor<0, 144>([&](auto tc) {
      constexpr int t = decltype(tc)::value;
      sfor<CS.first[t], CS.first[t + 1]>([&](auto nc) {
        constexpr int n = decltype(nc)::value;
        const v2u l_ = *(LAS v2u*)(aT + 64 * (59 - 8 * (n & 7) - (n >> 3))), h_ = *(LAS v2u*)(aT2 + 64 * (59 - 8 * (n & 7) - (n >> 3)));
        A[n] = v4u{l_.x, l_.y, h_.x, h_.y};
        Fm[n] = ld128(aF + 64 * (8 * (n & 7) - (n >> 3) + 3));
        SB();
      });
      MFMA16(acc[CS.sp[t] + CS.d[t]], A[CS.res[t] * 8 + CS.sp[t]], Fm[CS.res[t] * 8 + CS.d[t]]);
      if constexpr (t == TFP) GLD32(fw, vfw_, rW, 0u);
      if constexpr (t == TFW) {
        VMW(cv_sum(TFP, t)); PIN(fw);
        if (RARE(!__all(fw == tag))) { SPIN(SPIN_CAP) { GLD32(fw, vfw_, rW, 0u); VMW(0); PIN(fw); if (__all(fw == tag)) break; } }
      }
      static_assert(cv_sum(0, TFW) <= W1R - W1D && cv_sum(0, 144) == NC, "W2 loads start behind the flag check");
      if constexpr (cv_at(t)) {
        constexpr int n = cv_sum(0, t);
        if constexpr (n < W1R - W1D) dma_p<0>(mW1 + (W1D + n) * 1024, v1, rW1, sW1 + (W1D + n) * 8 * 3584); else W2LD(n - (W1R - W1D));
      }
      if constexpr (t == TFB) { VMW(0); BARE(); if (tid == 0) gst32(rW, tag, b * 128 + OFF_FB); }
      if constexpr (t >= TQ && t < TQ + 16 && ((t - TQ) & 1) == 0) {
        constexpr int i = (t - TQ) >> 1;
        if constexpr (i < 4) gldv_s<0>(qg[i], vL, rW, OFF_Q + b * 16384 + i * 4096 + w * 1024); else gldv_s<0>(qs[i - 4], vL, rW, OFF_QS + (i - 4) * 4096 + w * 1024);
      }
      if constexpr (t == TQW) {
        VMW(cv_sum(TQ + 16, TQW + 1));
#pragma unroll
        for (int i = 0; i < 4; ++i) { PIN(qg[i]); PIN(qs[i]); }
        u32 d = 0;
#pragma unroll
        for (int i = 0; i < 4; ++i) d |= ((qg[i].y ^ tag) | (qg[i].w ^ tag)) | ((qs[i].y ^ tag) | (qs[i].w ^ tag));
        if (RARE(__any(d != 0u))) {
          const u32 sq = OFF_Q + b * 16384 + w * 1024;
          SPIN(SPIN_CAP) {
            gldv_s<0>(qg[0], vL, rW, sq); gldv_s<0>(qg[1], vL, rW, sq + 4096); gldv_s<0>(qg[2], vL, rW, sq + 8192); gldv_s<0>(qg[3], vL, rW, sq + 12288);
            gldv_s<0>(qs[0], vL, rW, OFF_QS + w * 1024); gldv_s<0>(qs[1], vL, rW, OFF_QS + 4096 + w * 1024); gldv_s<0>(qs[2], vL, rW, OFF_QS + 8192 + w * 1024); gldv_s<0>(qs[3], vL, rW, OFF_QS + 12288 + w * 1024);
            VMW(0);
            d = 0;
#pragma unroll
            for (int i = 0; i < 4; ++i) { PIN(qg[i]); PIN(qs[i]); d |= ((qg[i].y ^ tag) | (qg[i].w ^ tag)) | ((qs[i].y ^ tag) | (qs[i].w ^ tag)); }
            if (!__any(d != 0u)) break;
          }
        }
        qv[0] = v4u{qg[0].x, qg[0].z, qg[1].x, qg[1].z}; qv[1] = v4u{qg[2].x, qg[2].z, qg[3].x, qg[3].z};
      }
      if constexpr (t > TQW + 1 && (t - TQW - 2) % ((142 - TQW) / 8) == 0 && (t - TQW - 2) / ((142 - TQW) / 8) < 8) {
        constexpr int i = (t - TQW - 2) / ((142 - TQW) / 8);
        const u32 s = qs[i >> 1][(i & 1) * 2];
        const float m = flo(s), rs = __builtin_amdgcn_rcpf(fhi(s));
#pragma unroll
        for (int cq = 0; cq < 2; ++cq) { nq[cq][i] = (((i & 1) ? fhi(qv[cq][i >> 1]) : flo(qv[cq][i >> 1])) - m) * rs; PIN(nq[cq][i]); }
      }
      SB();
    });
    asm volatile("s_nop 7" : "+v"(acc[0]), "+v"(acc[1]), "+v"(acc[2]), "+v"(acc[3]), "+v"(acc[4]), "+v"(acc[5]), "+v"(acc[6]), "+v"(acc[7]));
    const v4u kvp0 = ld128(L_KV + 512 + tid * 16), kvp1 = ld128(L_KV + 4608 + 512 + tid * 16);
    VMW(NC - (W1R - W1D));
    BAR();
#pragma unroll
    for (int i = 0; i < 8; ++i) st128(L_RED + w * 10240 + i * 1280 + G * 320 + cc * 16, __builtin_bit_cast(v4u, acc[i]));
    float kd[2][8];
#pragma unroll
    for (int e = 0; e < 4; ++e) { kd[0][2 * e] = flo(kvp0[e]) * flo(Dw); kd[0][2 * e + 1] = fhi(kvp0[e]) * flo(Dw); kd[1][2 * e] = flo(kvp1[e]) * fhi(Dw); kd[1][2 * e + 1] = fhi(kvp1[e]) * fhi(Dw); }
#pragma unroll
    for (int e = 0; e < 8; ++e) { PIN(kd[0][e]); PIN(kd[1][e]); }
    BAR();
    const u32 ar = L_RED + (tid >> 5) * 1280 + (tid & 1) * 640 + ((tid >> 1) & 15) * 16;
#pragma unroll
    for (int cq = 0; cq < 2; ++cq) {
      const f32x4 a0 = ldf4(ar + cq * 20480) + ldf4(ar + cq * 20480 + 10240), a1 = ldf4(ar + cq * 20480 + 320) + ldf4(ar + cq * 20480 + 10240 + 320);
      v4u yo, xo;
#pragma unroll
      for (int e = 0; e < 4; ++e) {
        const float c0 = e < 2 ? a0[2 * e] : a1[2 * e - 4], c1 = e < 2 ? a0[2 * e + 1] : a1[2 * e - 3];
        const u32 yp = pk(c0 + kd[cq][2 * e], c1 + kd[cq][2 * e + 1]);
        yo[e] = yp;
        xo[e] = nosent(pk(flo(qv[cq][e]) + nq[cq][2 * e] * flo(yp), fhi(qv[cq][e]) + nq[cq][2 * e + 1] * fhi(yp)));
      }
      gst(rW, xo, vg, OFF_X1 + b * 128 + cq * 64);
      gst(rW, yo, vg, OFF_Y + b * 128 + cq * 64);
    }
  }
  v4u xr[2];
  sfor<0, 2>([&](auto lc) {
    constexpr int L = decltype(lc)::value;
    constexpr u32 offX = L ? OFF_X3 : OFF_X1, offH = OFF_H + L * 64 * HG;
    int T1o = T1, fto = ft, cto = ct, hfo = hf;
    asm volatile("" : "+s"(T1o), "+s"(fto), "+s"(cto), "+s"(hfo));
    const int T1 = T1o, ft = fto, ct = cto, hf = hfo, gA = 4 * T1 + hf, par = ft & 1;
    const u32 sX = offX + 4 * T1 * GSZ + w * 7168, mX = L_X + w * 7168;
#define XP(n) dma_h<((n) % 7) & 3>(mX + ((2 + (n) / 7) ^ par) * GSZ + (((n) % 7) >> 2) * 4096, vL, rW, sX + ((2 + (n) / 7) ^ par) * GSZ + (((n) % 7) >> 2) * 4096)
#define XDMA(lg) do { const u32 m_ = mX + (lg) * GSZ, s_ = sX + (lg) * GSZ; \
      DMA2(m_, vL, rW, s_); DMA2(m_ + 2048, vL, rW, s_ + 2048); DMA2(m_ + 4096, vL, rW, s_ + 4096); dma_s<0>(m_ + 6144, vL, rW, s_ + 6144); } while (0)
    if constexpr (L == 0) {
      LANE_VARS;
      const v4u ones4 = {~0u, ~0u, ~0u, ~0u};
      gst(rW, ones4, vL, OFF_X3 + gA * GSZ + ct * 4096 + w * 1024); gst(rW, ones4, vL, OFF_X3 + (gA + 2) * GSZ + ct * 4096 + w * 1024);
      const u32 vfb = (w * 56 + (lane < 56 ? lane : 0)) * 128 + OFF_FB;
      u32 fb; GLD32(fb, vfb, rW, 0u);
      BARE();
      const u32 a1w = L_W1 + (w >> 1) * 57344 + (8 * h2 + q4) * 128 + (w & 1) * 64 + blk * 32 + p4 * 8;
#pragma unroll
      for (int sg = 0; sg < 4; ++sg) {
        const u32 as = a1w + ((sg + w) & 3) * 14336;
#pragma unroll
        for (int i = 0; i < 7; ++i) W1[sg * 7 + i] = tr2(as + i * 2048, as + i * 2048 + 512);
        if (sg > 0) {
#pragma unroll
          for (int i = 0; i < 7; ++i) toa(W1[sg * 7 - 7 + i]);
        }
      }
#pragma unroll
      for (int i = 21; i < 28; ++i) toa(W1[i]);
      BAR();
      f32x16 cs;
      v4u one = {0x3f803f80u, 0x3f803f80u, 0x3f803f80u, 0x3f803f80u};
      asm volatile("s_nop 1" : "+v"(one));
      mf_avz(cs, W1[0], one);
#pragma unroll
      for (int i = 1; i < 20; ++i) mf_av(cs, W1[i], one);
      VMW(0); PIN(fb);
      if (RARE(!__all(fb == tag))) { SPIN(SPIN_CAP) { GLD32(fb, vfb, rW, 0u); VMW(0); PIN(fb); if (__all(fb == tag)) break; } }
      const u32 va = (lane < 56 ? lane : 0) * 128 + 124 + 3 * GSZ;
      u32 f; GLD32(f, va, rW, sX);
      XDMA(par);
#pragma unroll
      for (int i = 20; i < 28; ++i) mf_av(cs, W1[i], one);
      nop12(cs);
      if ((lane & 31) == 0) {
#pragma unroll
        for (int q = 0; q < 4; ++q) st128(L_CS + w * 128 + h2 * 64 + q * 16, __builtin_bit_cast(v4u, f32x4{cs[4 * q], cs[4 * q + 1], cs[4 * q + 2], cs[4 * q + 3]}));
      }
      VMW(7); PIN(f);
      if (!__all(f != ~0u)) {
        SPIN(SPIN_CAP) { GLD32(f, va, rW, sX); VMW(0); PIN(f); if (__all(f != ~0u)) break; }
        XDMA(par);
      }
      XDMA(1 ^ par);
    }
    const u32 mH = L_X + (w >> 1) * GSZ + (w & 1) * 14336;
    const u32 sHe = offH + gA * HG + w * GSZ + hf * 14336, sHl = offH + gA * HG + w * GSZ + (1 - hf) * 14336;
#define HP(n) dma_h<((n) % 14) & 3>(mH + (((n) % 28) / 14) * 2 * GSZ + (((n) % 14) >> 2) * 4096, vL, rW, ((((n) / 14) & 1) ? sHl : sHe) + ((n) / 28) * 2 * HG + (((n) % 14) >> 2) * 4096)
    u32 axl, asp, acs, aw1, amn, atr, aow, vst; v4u sel;
    {
      LANE_VARS;
      atr = (8 * h2 + q4) * 64 + blk * 32 + p4 * 8;
      axl = L_X + (L ? vL : atr); asp = L_SP + (lane & 31) * 16; acs = L_CS + w * 128 + h2 * 64;
      aw1 = lane < 16 ? L_SP + lane * 16 + w * 4 : L_DM + lane * 4; amn = L_MN + (lane & 31) * 4;
      aow = (16 * (lane >> 4) + 8 * (p4 >> 1) + q4) * 64 + w * 16 + (p4 & 1) * 8;
      vst = (lane < 56 ? lane : lane - 8) * 16;
      const u32 sv = (lane & 15) == blk ? 0x3f803f80u : 0u;
      sel = v4u{sv, sv, sv, sv}; PIN(sel);
    }
    auto xrd = [&](u32 a) { if constexpr (L) return ld128(a); else return tr2(a, a + 256); };
    f32x16 acc[2]; Epi E; u32 bad = 0;
    const u32 sS2 = OFF_S2 + ((L * 32 + 2 * T1 + hf) * 4 + w) * 1024;
    const u32 soH = offH + 4 * T1 * HG + w * GSZ + (par * 7 + (ft >> 1)) * 2048;
    auto mean_step = [&](auto kc, int sl) {
      constexpr int k = decltype(kc)::value;
      if constexpr (k == 0) E.pa = ldf4(asp + (sl & 1) * 512);
      if constexpr (k == 2) { E.s1 = add_v(E.pa[0], E.pa[1]); E.s2 = add_v(E.pa[2], E.pa[3]); }
      if constexpr (k == 3) E.mean = add_v(E.s1, E.s2);
      if constexpr (k == 4) {
        const u64 nan = __builtin_amdgcn_ballot_w64(E.mean != E.mean);
        E.mean = mul_v(E.mean, 1.0f / 448.0f);
        bad |= csel(nan, 1u << sl, 0u);
        E.so = csel(nan, OFF_DUMMY, soH + (sl ^ par) * HG); }
      if constexpr (k == 5) { E.mb = __uint_as_float(pk_v(E.mean, E.mean)); *(LAS float*)(amn + (sl ^ par) * 128) = E.mean; }
      if constexpr (k == 6) E.mb = __uint_as_float(shl16_v(__float_as_uint(E.mb)));
    };
    auto epi_step = [&](auto kc, const f32x16& ac, u32 sg) {
      constexpr int k = decltype(kc)::value;
      if constexpr (k == 0) E.c[0] = ldf4(acs);
      if constexpr (k == 1) E.c[1] = ldf4(acs + 16);
      if constexpr (k >= 4 && k <= 18 && (k & 1) == 0) E.o[((k - 4) >> 1) & 3] = relu_tag_v(E.mx, E.ng, sg);
      if constexpr (k >= 3 && k <= 17 && (k & 1) == 1) { E.mx = pk_v(E.t0, E.t1); E.ng = sgn_v(E.mx, 0x09090808u); }
      if constexpr (k >= 2 && k <= 16 && (k & 1) == 0) {
        constexpr int r = k - 2;
        E.t0 = fnma_v(E.mb, E.c[(r >> 2) & 1][r & 3], ac[r]); E.t1 = fnma_v(E.mb, E.c[(r >> 2) & 1][(r & 3) + 1], ac[r + 1]);
      }
      if constexpr (k == 5) E.c[0] = ldf4(acs + 32);
      if constexpr (k == 9) E.c[1] = ldf4(acs + 48);
      if constexpr (k == 11) gst(rW, E.o, vL, E.so);
      if constexpr (k == 18) gst(rW, E.o, vL, E.so + 1024);
    };
    auto sum_out = [&](f32x4& sx, int sl) { PIN(sx); *(LAS float*)(aw1 + (sl & 1) * 512) = sx[0]; *(LAS float*)(aw1 + (sl & 1) * 512 + 256) = sx[1]; };
    float q2[4]; u32 sqbad = 0;
    auto sq_step = [&](auto kc) {
      constexpr int k = decltype(kc)::value;
      if constexpr (k == 0) { const u32 a = L_X + aow + hf * GSZ + ct * 4096; xr[0] = tr2(a, a + 256); }
      if constexpr (k == 1) { const u32 a = L_X + aow + (hf + 2) * GSZ + ct * 4096; xr[1] = tr2(a, a + 256); }
      if constexpr (k >= 3 && k <= 10) {
        constexpr int e = k - 3;
        if constexpr ((e & 3) == 0) { q2[2 * (e >> 2)] = mul_v(E.u0, E.u0); q2[2 * (e >> 2) + 1] = mul_v(E.u1, E.u1); }
        else { q2[2 * (e >> 2)] = fma_v(E.u0, E.u0, q2[2 * (e >> 2)]); q2[2 * (e >> 2) + 1] = fma_v(E.u1, E.u1, q2[2 * (e >> 2) + 1]); }
      }
      if constexpr (k >= 2 && k <= 9) { constexpr int e = k - 2; E.u0 = __uint_as_float(shl16_v(xr[e >> 2][e & 3])); E.u1 = __uint_as_float(and_v(xr[e >> 2][e & 3], 0xffff0000u)); }
      if constexpr (k == 11) { q2[0] = add_v(q2[0], q2[1]); q2[2] = add_v(q2[2], q2[3]); }
      if constexpr (k == 12) { q2[0] = oct_sum(q2[0]); PIN(q2[0]); }
      if constexpr (k == 13) { q2[2] = oct_sum(q2[2]); PIN(q2[2]); }
    };
    auto sq_pub = [&](bool force) {
      const u64 nan = force ? 0ull : __builtin_amdgcn_ballot_w64(q2[0] + q2[2] != q2[0] + q2[2]);
      sqbad = csel(nan, 1u, 0u);
      gst(rW, v4u{__float_as_uint(q2[0]), tag, __float_as_uint(q2[2]), tag}, vL & 127, csel(nan, OFF_DUMMY, sS2 + ct * 128));
    };
    v4u yv[2]; u32 fy;
    const u32 vfy = ((vL >> 4) & 31) * 128 + OFF_FY, vhb = (2 + (vL >> 9)) * GSZ + (((vL >> 4) & 7) < 7 ? ((vL >> 4) & 7) : 0) * 1024 + 1020;
    {
      VMW(7);
      BAR();
      if constexpr (L == 0) { if (tid == 64) { gst32(rW, tag, b * 128 + OFF_FY); if (b == 0) gst32(rW, tag, OFF_EPOCH); } }
      v4u x[112]; f32x4 sx; u32 as[16];
      sfor<0, 116>([&](auto sc) {
        constexpr int s = decltype(sc)::value;
        if constexpr (s >= 4) { constexpr int m = s - 4; if constexpr (m % 28 == 0) mf_avz(acc[(m / 28) & 1], W1[0], x[m]); else mf_av(acc[(m / 28) & 1], W1[m % 28], x[m]); SB(); }
        if constexpr (s < 112) {
          if constexpr (s % 7 == 0) as[s / 7] = axl + ((s / 28) ^ par) * GSZ + (((s / 7) + w) & 3) * 7168;
          x[s] = xrd(as[s / 7] + (s % 7) * 1024);
          SB();
        }
        if constexpr (s >= 4) {
          constexpr int m = s - 4, sl = m / 28, i = m % 28;
          if constexpr (i == 6) { mf16z(sx, sel, x[m - 6]); sfor<1, 7>([&](auto fc) { MFMA16(sx, sel, x[m - 6 + decltype(fc)::value]); }); }
          if constexpr (i == 12) sum_out(sx, sl);
          if constexpr (sl < 2 ? i == 20 : m == 67) VMW(L == 0 ? g1_sum(L, 7 * sl, m) : sl == 0 ? g1_sum(L, 0, m) : g1_sum(L, M_XB + 7 * sl, m));
          if constexpr (L == 1) {
            if constexpr (m == M_HB) GLD32(fy, vhb, rW, sX);
            if constexpr (m == M_XB) {
              VMW(g1_sum(L, M_HB + 1, m)); PIN(fy);
              if (RARE(!__all(fy != ~0u))) { SPIN(SPIN_CAP) { GLD32(fy, vhb, rW, sX); VMW(0); PIN(fy); if (__all(fy != ~0u)) break; } }
            }
            if constexpr (m >= M_XB && m < M_XB + 14) XP(m - M_XB);
          }
          if constexpr (i == 20) BARE();
          if constexpr (i >= 21) mean_step(IC<i - 21>{}, sl);
          if constexpr (sl > 0 && i >= 1 && i <= 19) epi_step(IC<i - 1>{}, acc[(sl - 1) & 1], sl == 3 ? smB : sm);
          if constexpr (L == 0) {
            if constexpr (m < 14) XP(m);
            if constexpr (w2_at(m)) W2LD(W2D + (m - M0) / W2K);
            if constexpr (m == M_FY) GLD32(fy, vfy, rW, ct * 4096);
            if constexpr (m == M_Y) {
              VMW(g1_sum(L, M_FY + 1, m)); PIN(fy);
              if (RARE(!__all(fy == tag))) { SPIN(SPIN_CAP) { GLD32(fy, vfy, rW, ct * 4096); VMW(0); PIN(fy); if (__all(fy == tag)) break; } }
              const u32 so = OFF_Y + gA * GSZ + ct * 4096 + w * 1024;
              dma_s<0>(L_R2 + w * 1024, vL, rW, so); dma_s<0>(L_R2 + 4096 + w * 1024, vL, rW, so + 2 * GSZ);
            }
            if constexpr (m == 72) sq_step(IC<0>{});
            if constexpr (m >= 77 && m < 90) sq_step(IC<m - 76>{});
            if constexpr (m == M_SQ) sq_pub(false);
            if constexpr (m == 95) VMW(g1_sum(L, M_Y + 1, m));
            if constexpr (m == 106) { yv[0] = tr2(L_R2 + aow, L_R2 + aow + 256); yv[1] = tr2(L_R2 + 4096 + aow, L_R2 + 4096 + aow + 256); }
          }
          if constexpr (sl == 3 && (i & 1) == 0) HP(i >> 1);
        }
        SB();
      });
    }
    BAR();
    if (RARE(bad)) {
      asm volatile("s_nop 11");
      sfor<0, 19>([&](auto kc) { epi_step(kc, acc[1], smB); });
      for (int sl = 0; sl < 4; ++sl) {
        if (!((bad >> sl) & 1u)) continue;
        for (int tries = 0; tries < 64; ++tries) {
          VMW(0); BAR(); XDMA(sl ^ par); VMW(0); BAR();
          const u32 ax = axl + (sl ^ par) * GSZ;
          f32x4 sx; f32x16 ar;
          sfor<0, 28>([&](auto kc) {
            constexpr int k = decltype(kc)::value;
            const v4u x = xrd(ax + (((k / 7) + w) & 3) * 7168 + (k % 7) * 1024);
            if constexpr (k == 0) mf_avz(ar, W1[0], x); else mf_av(ar, W1[k], x);
            if constexpr (k == 0) mf16z(sx, sel, x); else if constexpr (k < 7) MFMA16(sx, sel, x);
            SB();
          });
          sum_out(sx, sl);
          BAR();
          bad &= ~(1u << sl);
          sfor<0, 7>([&](auto kc) { mean_step(kc, sl); });
          if (tries == 63) E.so = soH + (sl ^ par) * HG;
          nop12(ar);
          sfor<0, 19>([&](auto kc) { epi_step(kc, ar, sl >= 2 ? smB : sm); });
          if (!((bad >> sl) & 1u)) break;
        }
      }
      VMW(0); BAR();
      sfor<0, 14>([&](auto nc) { HP(decltype(nc)::value); });
      VMW(0);
      E.so = OFF_DUMMY;
    }
    if constexpr (L == 0) {
      if (RARE(sqbad)) {
        for (int tries = 0; tries < 64 && sqbad; ++tries) {
          const u32 so = OFF_X1 + gA * GSZ + ct * 4096, m = L_R2 + w * 8192;
          DMA2(m, vL, rW, so); DMA2(m + 2048, vL, rW, so + 2048); DMA2(m + 4096, vL, rW, so + 2 * GSZ); DMA2(m + 6144, vL, rW, so + 2 * GSZ + 2048);
          VMW(0);
          xr[0] = tr2(m + aow, m + aow + 256); xr[1] = tr2(m + 4096 + aow, m + 4096 + aow + 256);
          sfor<2, 14>([&](auto kc) { sq_step(kc); });
          sq_pub(tries == 63);
        }
      }
    }
    v4u g2; float rsv[2], mn[2];
#define G2OK __all(g2.y == tag && g2.w == tag)
    auto wait1 = [&](u32 so) {
      PIN(g2);
      if (RARE(!G2OK)) { SPIN(SPIN_CAP) { gld1(g2, vst, rW, so); VMW(0); PIN(g2); if (G2OK) break; } }
    };
    auto rs_step = [&](auto kc) {
      constexpr int k = decltype(kc)::value;
      if constexpr (k == 10) {
        VMW(g2_sum(L, 0, U_S2 + 1, 14) + 1 + g2_sum(L, 1, 0, 5)); wait1(sS2);
        const u32 a = L_MN + hf * 128 + w * 32 + ((vL >> 2) & 28);
        mn[0] = *(LAS float*)a; mn[1] = *(LAS float*)(a + 256);
        rsv[0] = vL < 896 ? __uint_as_float(g2.x) : 0.f; rsv[1] = vL < 896 ? __uint_as_float(g2.z) : 0.f; PIN(rsv[0]); PIN(rsv[1]);
      }
      if constexpr (k == 12) { rsv[0] = oct_sum(rsv[0]); PIN(rsv[0]); }
      if constexpr (k == 14) { rsv[1] = oct_sum(rsv[1]); PIN(rsv[1]); }
      if constexpr (k >= 16) {
#pragma unroll
        for (int c = 0; c < 2; ++c) {
          float& v = rsv[c];
          if constexpr (k == 16) v = mul_v(v, 1.0f / 448.0f);
          if constexpr (k == 17) v = fnma_v(mn[c], mn[c], v);
          if constexpr (k == 18) { v = fmaxf(v, 0.f); PIN(v); }
          if constexpr (k == 19 || k == 22 || k == 25) v = __uint_as_float(pk_v(v, v));
          if constexpr (k == 20 || k == 23 || k == 26) v = __uint_as_float(shl16_v(__float_as_uint(v)));
          if constexpr (k == 21) v = add_v(v, 1e-5f);
          if constexpr (k == 24) { v = __builtin_amdgcn_sqrtf(v); PIN(v); }
          if constexpr (k == 27) { v = __builtin_amdgcn_rcpf(c ? -v : v); PIN(v); }
        }
      }
    };
    const u32 sST = OFF_ST + (gA * 4 + w) * 1024;
    u32 ar2, aw2, vout; f32x4 fa[4]; float xv[8]; f32x16 g2a[2][2];
    { LANE_VARS; ar2 = L_R2 + (8 * w + (lane & 7)) * 272 + (lane >> 3) * 32; aw2 = L_R2 + w * 8704 + (lane & 31) * 272 + h2 * 16; vout = (8 * w + (lane & 7)) * 896 + (lane >> 3) * 16; }
    u32 fixA = 0;
    auto ea_step = [&](auto kc) {
      constexpr int k0 = decltype(kc)::value, k = k0 - EA0;
      if constexpr (k0 >= 3 && k0 < 7) {
#pragma unroll
        for (int c = 0; c < 2; ++c) { constexpr int q = k0 - 3; const f32x16& s = g2a[0][c];
          st128(aw2 + fixA + c * 128 + q * 32, __builtin_bit_cast(v4u, f32x4{s[4 * q], s[4 * q + 1], s[4 * q + 2], s[4 * q + 3]})); }
      }
      if constexpr (k == 2) { asm volatile("s_waitcnt lgkmcnt(1)\n\ts_barrier" ::: "memory"); fa[0] = ldf4(ar2); fa[1] = ldf4(ar2 + 8704); }
      if constexpr (k == 3) { fa[2] = ldf4(ar2 + 17408); fa[3] = ldf4(ar2 + 26112); }
      if constexpr (k == 5 || k == 6) { constexpr int e = 2 * (k - 5); xv[e] = add_v(fa[0][e], fa[1][e]); xv[e + 1] = add_v(fa[0][e + 1], fa[1][e + 1]); }
      if constexpr (k == 7) { fa[0] = ldf4(ar2 + 16); fa[1] = ldf4(ar2 + 8720); }
      if constexpr (k == 7 || k == 8) { constexpr int e = 2 * (k - 7); xv[e] = add_v(xv[e], fa[2][e]); xv[e + 1] = add_v(xv[e + 1], fa[2][e + 1]); }
      if constexpr (k == 9 || k == 10) { constexpr int e = 2 * (k - 9); xv[e] = add_v(xv[e], fa[3][e]); xv[e + 1] = add_v(xv[e + 1], fa[3][e + 1]); }
      if constexpr (k == 11) { fa[2] = ldf4(ar2 + 17424); fa[3] = ldf4(ar2 + 26128); }
      if constexpr (k == 11 || k == 12) { constexpr int e = 2 * (k - 11); xv[4 + e] = add_v(fa[0][e], fa[1][e]); xv[5 + e] = add_v(fa[0][e + 1], fa[1][e + 1]); }
      if constexpr (k == 13 || k == 14) { constexpr int e = 2 * (k - 13); xv[4 + e] = add_v(xv[4 + e], fa[2][e]); xv[5 + e] = add_v(xv[5 + e], fa[2][e + 1]); }
      if constexpr (k == 15 || k == 16) { constexpr int e = 2 * (k - 15); xv[4 + e] = add_v(xv[4 + e], fa[3][e]); xv[5 + e] = add_v(xv[5 + e], fa[3][e + 1]); }
      if constexpr (k >= 14 && k <= 17) { constexpr int e = k - 14; xv[2 * e] = fma_v(xv[2 * e], rsv[0], E.u0); xv[2 * e + 1] = fma_v(xv[2 * e + 1], rsv[0], E.u1); }
      if constexpr (k >= 13 && k <= 16) { constexpr int e = k - 13; E.u0 = __uint_as_float(shl16_v(xr[0][e])); E.u1 = __uint_as_float(and_v(xr[0][e], 0xffff0000u)); }
      if constexpr (k >= 18 && k <= 21) { constexpr int e = k - 18; xr[0][e] = pk_v(xv[2 * e], xv[2 * e + 1]); }
      if constexpr (L == 0) {
        if constexpr (k == 18) { E.s1 = add_v(xv[0], xv[1]); E.s2 = mul_v(xv[0], xv[0]); E.t0 = add_v(xv[2], xv[3]); E.t1 = mul_v(xv[1], xv[1]); }
        if constexpr (k == 19) { E.s1 = add_v(E.s1, xv[4]); E.s2 = fma_v(xv[2], xv[2], E.s2); E.t0 = add_v(E.t0, xv[5]); E.t1 = fma_v(xv[3], xv[3], E.t1); }
        if constexpr (k == 20) { E.s1 = add_v(E.s1, xv[6]); E.s2 = fma_v(xv[4], xv[4], E.s2); E.t0 = add_v(E.t0, xv[7]); E.t1 = fma_v(xv[5], xv[5], E.t1); }
        if constexpr (k == 21) { E.s1 = add_v(E.s1, E.t0); E.s2 = fma_v(xv[6], xv[6], E.s2); E.t1 = fma_v(xv[7], xv[7], E.t1); }
        if constexpr (k == 22) { E.s2 = add_v(E.s2, E.t1); E.s1 = oct_sum(E.s1); PIN(E.s1); }
        if constexpr (k == 24) { E.s2 = oct_sum(E.s2); PIN(E.s2); }
        if constexpr (k0 == 28 + 2 * U_EA + 1) gst(rW, v4u{__float_as_uint(E.s1), tag, __float_as_uint(E.s2), tag}, vL & 127, sST + ct * 128);
      } else {
        if constexpr (k0 == 28 + 2 * U_EA + 1) gstp(mk(P.out, GM), xr[0], vout, gA * GSZ + ct * 128);
      }
    };
    sfor<0, 2>([&](auto hc_) {
      constexpr int half = decltype(hc_)::value;
      f32x16& a0 = g2a[half][0]; f32x16& a1 = g2a[half][1]; u32 chk = 0, c01, c23;
      const u32 smh = half ? smB : sm;
      auto mm = [&](auto nc, f32x16& A_, const v4u& hx) {
        constexpr int n = decltype(nc)::value;
        if constexpr (n < 2) mf_avz(A_, W2[n], hx); else if constexpr (n < 36) mf_av(A_, W2[n], hx); else mf_vv(A_, W2[n], hx);
      };
      sfor<0, 2>([&](auto qc) {
        constexpr int sq = decltype(qc)::value, st = half * 2 + sq;
        const u32 ah = mH + sq * 2 * GSZ + vL;
        {
          v4u hfr[14];
          sfor<0, 16>([&](auto ic) {
            constexpr int i = decltype(ic)::value;
            if constexpr (i >= 2) { mm(IC<sq * 28 + 2 * (i - 2)>{}, a0, hfr[i - 2]); SB(); }
            if constexpr (i < 14) { if constexpr ((i & 1) == 0) VMW(h_wait(L, st, i + 1) - g2_ops(L, st, i - 2)); hfr[i] = ld128(ah + i * 1024); SB(); }
            if constexpr (i >= 2) {
              constexpr int u = i - 2;
              if constexpr (st == 0 && (2 * u) % 3 != 2) epi_step(IC<2 * u - (2 * u + 1) / 3>{}, acc[1], smB);
              if constexpr (st == 1) rs_step(IC<2 * u>{});
              if constexpr (st >= 2) ea_step(IC<(st - 2) * 28 + 2 * u>{});
              c01 = off_v(hfr[u].x, hfr[u].y, smh); c23 = off_v(hfr[u].z, hfr[u].w, smh);
              SB();
              mm(IC<sq * 28 + 2 * u + 1>{}, a1, hfr[u]);
              if constexpr (st == 0 && (2 * u + 1) % 3 != 2) epi_step(IC<2 * u + 1 - (2 * u + 2) / 3>{}, acc[1], smB);
              if constexpr (st == 1) rs_step(IC<2 * u + 1>{});
              if constexpr (st >= 2) ea_step(IC<(st - 2) * 28 + 2 * u + 1>{});
              if constexpr (st == 3 && u == 6) BARE();
              chk = or3_v(chk, c01, c23);
              if constexpr (st == 0 && u == U_S2) gld1(g2, vst, rW, sS2);
              if constexpr (L == 0 && st == 3 && u == U_PA) gld1(g2, vst, rW, sST);
              if constexpr (st < 3) HP(14 * (st + 1) + u);
            }
            SB();
          });
        }
      });
      if constexpr (half == 1) nop12(a0, a1);
      LANE_VARS;
      auto r2_out = [&](const f32x16& c0, const f32x16& c1) {
        const u32 a = aw2;
#pragma unroll
        for (int q = 0; q < 4; ++q) {
          st128(a + q * 32, __builtin_bit_cast(v4u, f32x4{c0[4 * q], c0[4 * q + 1], c0[4 * q + 2], c0[4 * q + 3]}));
          st128(a + 128 + q * 32, __builtin_bit_cast(v4u, f32x4{c1[4 * q], c1[4 * q + 1], c1[4 * q + 2], c1[4 * q + 3]}));
        }
      };
      if constexpr (half == 1) r2_out(a0, a1);
      if (RARE(__any((chk & 0x80008000u) != 0u))) {
        const u32 ah = mH + vL;
        SPIN(SPIN_CAP) {
          VMW(0);
          sfor<0, 28>([&](auto nc) { HP(half * 28 + decltype(nc)::value); });
          VMW(0);
          u32 o = 0;
          sfor<0, 28>([&](auto ic) { constexpr int i = decltype(ic)::value; const v4u z = ld128(ah + (i / 14) * 2 * GSZ + (i % 14) * 1024); o |= ((z.x ^ smh) | (z.y ^ smh)) | ((z.z ^ smh) | (z.w ^ smh)); PIN(o); });
          if (!__any((o & 0x80008000u) != 0u)) break;
        }
        f32x16 c0, c1;
        sfor<0, 28>([&](auto ic) { constexpr int i = decltype(ic)::value; const v4u z = ld128(ah + (i / 14) * 2 * GSZ + (i % 14) * 1024); mm(IC<2 * i>{}, c0, z); mm(IC<2 * i + 1>{}, c1, z); SB(); });
        nop12(c0, c1);
        r2_out(c0, c1);
        if constexpr (half == 0) { sfor<28, 42>([&](auto nc) { HP(decltype(nc)::value); }); VMW(0); fixA = L_DM2 - L_R2 - w * 8704; }
      }
      if constexpr (half == 1) {
        float q3[2];
        const u32 vx3 = (lane >> 4) * 1024 + ((lane >> 3) & 1) * 512 + (8 * w + (lane & 7)) * 16;
        auto gate = [&](int hh) {
          float m, sd; ln_consts(oct_sum(lane < 56 ? __uint_as_float(g2.x) : 0.f), oct_sum(lane < 56 ? __uint_as_float(g2.z) : 0.f), m, sd);
          const float rs = __builtin_amdgcn_rcpf(sd);
          v4u x3;
#pragma unroll
          for (int e = 0; e < 4; ++e) {
            const float xa = flo(xr[hh][e]), xb = fhi(xr[hh][e]);
            x3[e] = nosent(pk(xa + (xa - m) * (rs * flo(yv[hh][e])), xb + (xb - m) * (rs * fhi(yv[hh][e]))));
          }
          gst(rW, x3, vx3, OFF_X3 + (gA + 2 * hh) * GSZ + ct * 4096);
          xr[hh] = x3;
          q3[hh] = dot2(x3.x, x3.x, dot2(x3.y, x3.y, 0.f)) + dot2(x3.z, x3.z, dot2(x3.w, x3.w, 0.f));
        };
        if constexpr (L == 0) { VMW(0); wait1(sST); gate(0); }
        BAR();
        const f32x4 f0 = (ldf4(ar2) + ldf4(ar2 + 8704)) + (ldf4(ar2 + 17408) + ldf4(ar2 + 26112));
        const f32x4 f1 = (ldf4(ar2 + 16) + ldf4(ar2 + 8720)) + (ldf4(ar2 + 17424) + ldf4(ar2 + 26128));
        const v4u x = xr[1]; const float r = rsv[1];
        const float z[8] = {flo(x.x) + f0[0] * r, fhi(x.x) + f0[1] * r, flo(x.y) + f0[2] * r, fhi(x.y) + f0[3] * r, flo(x.z) + f1[0] * r, fhi(x.z) + f1[1] * r, flo(x.w) + f1[2] * r, fhi(x.w) + f1[3] * r};
        xr[1] = v4u{pk(z[0], z[1]), pk(z[2], z[3]), pk(z[4], z[5]), pk(z[6], z[7])};
        if constexpr (L == 1) gstp(mk(P.out, GM), xr[1], vout, (gA + 2) * GSZ + ct * 128);
        else {
          const float s1 = oct_sum(((z[0] + z[1]) + (z[2] + z[3])) + ((z[4] + z[5]) + (z[6] + z[7])));
          const float s2 = oct_sum(((z[0] * z[0] + z[1] * z[1]) + (z[2] * z[2] + z[3] * z[3])) + ((z[4] * z[4] + z[5] * z[5]) + (z[6] * z[6] + z[7] * z[7])));
          gst(rW, v4u{__float_as_uint(s1), tag, __float_as_uint(s2), tag}, vL & 127, sST + 2 * 4 * 1024 + ct * 128);
          const u32 vh = h2 * GSZ + ((lane & 7) < 7 ? (lane & 7) : 0) * 1024 + 1020, sh = OFF_X3 + 4 * T1 * GSZ + w * 7168, m2 = L_X + w * 7168;
          u32 hint = ~0u;
          __builtin_amdgcn_s_sleep(8);
          SPIN(SPIN_CAP) {
            gld1(g2, vst, rW, sST + 2 * 4 * 1024); GLD32(hint, vh, rW, sh); VMW(0);
            PIN(g2); PIN(hint);
            if (G2OK) break;
          }
#define XDMA3(lg) do { const u32 m_ = m2 + (lg) * GSZ, s_ = sh + (lg) * GSZ; \
      DMA2(m_, vL, rW, s_); DMA2(m_ + 2048, vL, rW, s_ + 2048); DMA2(m_ + 4096, vL, rW, s_ + 4096); dma_s<0>(m_ + 6144, vL, rW, s_ + 6144); } while (0)
          const bool early = __all(hint != ~0u);
          if (early) XDMA3(par);
          gate(1);
          if (!early) {
            SPIN(SPIN_CAP) { GLD32(hint, vh, rW, sh); VMW(0); PIN(hint); if (__all(hint != ~0u)) break; }
            XDMA3(par);
          }
          XDMA3(1 ^ par);
          gst(rW, v4u{__float_as_uint(oct_sum(q3[0])), tag, __float_as_uint(oct_sum(q3[1])), tag}, vL & 127, OFF_S2 + ((32 + 2 * T1 + hf) * 4 + w) * 1024 + ct * 128);
        }
      }
    });
  });
}
}

extern "C" void kernel_launch(void* const* d_in, const int* in_sizes, int n_in, void* d_out, int out_size, void* d_ws, size_t ws_size, hipStream_t stream) {
  Params p;
  p.q = (const u16*)d_in[0]; p.k = (const u16*)d_in[1]; p.v = (const u16*)d_in[2]; p.sk = (const u16*)d_in[3]; p.h = (const u16*)d_in[4];
  p.Dv = (const u16*)d_in[5]; p.w1 = (const u16*)d_in[7]; p.w2 = (const u16*)d_in[8]; p.out = (u16*)d_out;
  p.ws = (char*)d_ws + ((ws_size - WS_USED) & ~(size_t)0x1fffff);
  (void)hipFuncSetAttribute((const void*)h3ffn_kernel, hipFuncAttributeMaxDynamicSharedMemorySize, LDS_BYTES);
  h3ffn_kernel<<<256, 256, LDS_BYTES, stream>>>(p);
}
```
